```python
import torch

def _softmax(x, axis=-1):
    return torch.softmax(x, dim=axis)
def _gelu(x):
    return torch.nn.functional.gelu(x, approximate="tanh")
def _silu(x):
    return torch.nn.functional.silu(x)
def _sigmoid(x):
    return torch.sigmoid(x)
def _layernorm(x, axis=-1, eps=1e-5):
    m = x.mean(dim=axis, keepdim=True)
    v = x.var(dim=axis, keepdim=True, unbiased=False)
    return (x - m) / torch.sqrt(v + eps)
def _scatter_add(tgt, idx, src, axis=0):
    return tgt.clone().index_add_(axis, idx, src)
def _segment_sum(src, seg, n):
    out = torch.zeros((n,) + src.shape[1:], dtype=src.dtype)
    return out.index_add_(0, seg, src)
def _pad(x, pw):
    flat = tuple(v for pair in reversed(pw) for v in pair)
    return torch.nn.functional.pad(x, flat)

kernel_name = 'concat_heads__sliding_block_cached__compressed_kv_cla__decode'

def setup_inputs():

    _w = (lambda a: (lambda t: t.to(torch.bfloat16) if t.is_floating_point() else t)(torch.as_tensor(a)))
    def _A_setup():
        B, Sq, D = 2, 1, 512
        Hq, Hkv, Sk, Dh = 32, 8, 8192, 128
        assert Hq % Hkv == 0, (Hq, Hkv)
        return {
            "x": _w((lambda *s: torch.randn(*s, dtype=torch.bfloat16) * 1.6)(B, Sq, D)),
            "k_cache": _w((lambda *s: torch.randn(*s, dtype=torch.bfloat16) * 1.6)(B, Hkv, Sk, Dh)),
            "v_cache": _w((lambda *s: torch.randn(*s, dtype=torch.bfloat16) * 0.05)(B, Hkv, Sk, Dh)),
            "wq": _w((lambda *s: torch.randn(*s, dtype=torch.bfloat16) * 1.6)(D, Hq * Dh) * (D ** -0.5)),
            "wo": _w((lambda *s: torch.randn(*s, dtype=torch.bfloat16) * 0.05)(Hq * Dh, D) * ((Hq * Dh) ** -0.5)),
        }
    _A_in = _A_setup()

    def _B_setup():
        B, H, Sq, Sk, D, M = 2, 32, 1, 8192, 128, 512
        Dc = 4 * D
        return {
            "x": _w((lambda *s: torch.randn(*s, dtype=torch.bfloat16) * 3.2)(B, Sq, M)),
            "c_kv": _w((lambda *s: torch.randn(*s, dtype=torch.bfloat16) * 0.1)(B, Sk, Dc)),
            "w_q": _w((lambda *s: torch.randn(*s, dtype=torch.bfloat16) * 6.4)(M, H, D) * (M ** -0.5)),
            "w_uk": _w((lambda *s: torch.randn(*s, dtype=torch.bfloat16) * 6.4)(Dc, H, D) * (Dc ** -0.5)),
            "w_uv": _w((lambda *s: torch.randn(*s, dtype=torch.bfloat16) * 0.1)(Dc, H, D) * (Dc ** -0.5)),
            "w_o": _w((lambda *s: torch.randn(*s, dtype=torch.bfloat16) * 0.1)(H, D, M) * ((H * D) ** -0.5)),
        }
    _B_in = _B_setup()

    return {
        "A_x": _A_in["x"],
        "A_k_cache": _A_in["k_cache"],
        "A_v_cache": _A_in["v_cache"],
        "A_wq": _A_in["wq"],
        "A_wo": _A_in["wo"],
        "B_x": _B_in["x"],
        "B_c_kv": _B_in["c_kv"],
        "B_w_q": _B_in["w_q"],
        "B_w_uk": _B_in["w_uk"],
        "B_w_uv": _B_in["w_uv"],
        "B_w_o": _B_in["w_o"],
    }

def reference(A_x, A_k_cache, A_v_cache, A_wq, A_wo, B_x, B_c_kv, B_w_q, B_w_uk, B_w_uv, B_w_o):
    def _A_ref(x, k_cache, v_cache, wq, wo):
        B, Sq, D = x.shape
        Hq, Dh = 32, 128
        Hkv, Sk = k_cache.shape[1], k_cache.shape[2]
        W = min(4096, max(256, Sk // 4))
        rep = Hq // Hkv
        q = (lambda x, *d: x.permute(*d))((x @ wq).reshape(B, Sq, Hq, Dh), 0, 2, 1, 3)
        k = (lambda x, n, axis: torch.repeat_interleave(x, n, dim=axis))(k_cache[:, :, Sk - W:, :], rep, axis=1)
        v = (lambda x, n, axis: torch.repeat_interleave(x, n, dim=axis))(v_cache[:, :, Sk - W:, :], rep, axis=1)
        scores = torch.einsum("bhqd,bhkd->bhqk", q, k) * (Dh ** -0.5)
        p = _softmax(scores, axis=-1)
        o = torch.einsum("bhqk,bhkd->bhqd", p, v)
        return (lambda x, *d: x.permute(*d))(o, 0, 2, 1, 3).reshape(B, Sq, Hq * Dh) @ wo

    def _B_ref(x, c_kv, w_q, w_uk, w_uv, w_o):
        B, Sq, M = x.shape[0], x.shape[1], x.shape[2]
        Sk = c_kv.shape[1]
        H, D = w_q.shape[1], w_q.shape[2]
        q = torch.einsum("bqm,mhd->bhqd", x, w_q)
        k = torch.einsum("bkc,chd->bhkd", c_kv, w_uk)
        v = torch.einsum("bkc,chd->bhkd", c_kv, w_uv)
        scores = torch.einsum("bhqd,bhkd->bhqk", q, k) * (D ** -0.5)
        off = Sk - Sq
        qi = torch.arange(Sq)[:, None]
        kj = torch.arange(Sk)[None, :]
        cmask = kj <= (qi + off)
        scores = torch.where(cmask[None, None], scores, -1e9)
        p = _softmax(scores, axis=-1)
        ctx = torch.einsum("bhqk,bhkd->bhqd", p, v)
        return torch.einsum("bhqd,hdm->bqm", ctx, w_o)

    a = _A_ref(A_x, A_k_cache, A_v_cache, A_wq, A_wo)
    b = _B_ref(B_x, B_c_kv, B_w_q, B_w_uk, B_w_uv, B_w_o)
    return torch.concatenate([a, b], axis=-1)
```

```cpp
#include <hip/hip_runtime.h>
#include <hip/hip_bf16.h>
#include <stdint.h>

typedef __attribute__((ext_vector_type(4))) unsigned u32x4;
typedef __attribute__((ext_vector_type(2))) unsigned u32x2;
typedef __attribute__((ext_vector_type(4))) float f32x4;
typedef __attribute__((ext_vector_type(2))) float f32x2;
typedef __attribute__((ext_vector_type(16))) float f32x16;
typedef __attribute__((ext_vector_type(8))) __bf16 b16x8;
typedef __attribute__((ext_vector_type(2))) __bf16 b16x2;
typedef __attribute__((ext_vector_type(4))) short s16x4;
typedef unsigned short u16;

#define DI __device__ __forceinline__

constexpr int NB = 2;
constexpr int DM = 512;
constexpr int HQ = 32, HKV = 8, SK = 8192, DH = 128, WIN = 2048;
constexpr int DC = 512;
constexpr int SA = 8;
constexpr int SB = 64;
constexpr float SCALE_LOG2 = 0.08838834764831845f * 1.4426950408889634f;

constexpr size_t WS_QA   = 0;
constexpr size_t WS_QABS = WS_QA + 2 * 4096 * 2;
constexpr size_t WS_PAO  = WS_QABS + 2 * 32 * 512 * 2;
constexpr size_t WS_PAML = WS_PAO + (size_t)2 * 32 * SA * 128 * 4;
constexpr size_t WS_PBO  = WS_PAML + (size_t)2 * 32 * SA * 2 * 4;
constexpr size_t WS_PBML = WS_PBO + (size_t)2 * SB * 32 * 512 * 4;
constexpr size_t WS_SLA  = WS_PBML + (size_t)2 * SB * 32 * 2 * 4;
constexpr size_t WS_SLB  = WS_SLA + (size_t)128 * 2 * 128 * 4;
constexpr size_t WS_END  = WS_SLB + (size_t)128 * 2 * 512 * 4;

DI float bf_lo(unsigned u) { return __builtin_bit_cast(float, u << 16); }
DI float bf_hi(unsigned u) { return __builtin_bit_cast(float, u & 0xffff0000u); }
DI float bf2f(u16 v) { return __builtin_bit_cast(float, (unsigned)v << 16); }
DI u16 f2bf(float f) { return __builtin_bit_cast(u16, __float2bfloat16(f)); }
DI unsigned pack_bf(float lo, float hi) { return (unsigned)f2bf(lo) | ((unsigned)f2bf(hi) << 16); }
DI float dot2(unsigned a, unsigned b, float acc) {
  return __builtin_amdgcn_fdot2_f32_bf16(__builtin_bit_cast(b16x2, a), __builtin_bit_cast(b16x2, b), acc, false);
}
DI u32x4 ldg16(const void* p) { return *reinterpret_cast<const u32x4*>(p); }
DI u32x4 ldg16nt(const void* p) { return __builtin_nontemporal_load(reinterpret_cast<const u32x4*>(p)); }
DI float shx(float v, int m) { return __shfl_xor(v, m, 64); }

DI void fma8(float* acc, float s, const u32x4& w) {
#pragma unroll
  for (int k = 0; k < 4; ++k) {
    acc[2 * k]     = fmaf(s, bf_lo(w[k]), acc[2 * k]);
    acc[2 * k + 1] = fmaf(s, bf_hi(w[k]), acc[2 * k + 1]);
  }
}

__global__ __launch_bounds__(256) void k1_qproj(const u16* __restrict__ xA, const u16* __restrict__ wq,
                                                const u16* __restrict__ xB, const u16* __restrict__ w_q,
                                                const u16* __restrict__ w_uk, u16* __restrict__ qA,
                                                u16* __restrict__ qabs) {
  __shared__ float xs[2][512];
  __shared__ float red[4][2][128];
  __shared__ float qs[2][128];
  const int tid = threadIdx.x, lane = tid & 63, wave = tid >> 6;
  const int blk = blockIdx.x;
  if (blk < 64) {
    const int n0 = blk * 64, cg = lane & 7, rl = lane >> 3;
    u32x4 w[16];
#pragma unroll
    for (int it = 0; it < 16; ++it) {
      const int row = it * 32 + wave * 8 + rl;
      w[it] = ldg16(wq + (size_t)row * 4096 + n0 + cg * 8);
    }
    for (int i = tid; i < 1024; i += 256) xs[i >> 9][i & 511] = bf2f(xA[i]);
    __syncthreads();
    float acc[2][8];
#pragma unroll
    for (int b = 0; b < 2; ++b)
#pragma unroll
      for (int k = 0; k < 8; ++k) acc[b][k] = 0.f;
#pragma unroll
    for (int it = 0; it < 16; ++it) {
      const int row = it * 32 + wave * 8 + rl;
      fma8(acc[0], xs[0][row], w[it]);
      fma8(acc[1], xs[1][row], w[it]);
    }
#pragma unroll
    for (int b = 0; b < 2; ++b)
#pragma unroll
      for (int k = 0; k < 8; ++k) {
        float v = acc[b][k];
        v += shx(v, 8); v += shx(v, 16); v += shx(v, 32);
        acc[b][k] = v;
      }
    if (lane < 8) {
#pragma unroll
      for (int b = 0; b < 2; ++b)
#pragma unroll
        for (int k = 0; k < 8; ++k) red[wave][b][cg * 8 + k] = acc[b][k];
    }
    __syncthreads();
    if (tid < 128) {
      const int b = tid >> 6, c = tid & 63;
      const float v = red[0][b][c] + red[1][b][c] + red[2][b][c] + red[3][b][c];
      qA[b * 4096 + n0 + c] = f2bf(v);
    }
  } else {
    const int j = blk - 64, h = j & 31, cq = j >> 5;
    const int dl = lane & 15, rl = lane >> 4;
    u32x4 w[32];
#pragma unroll
    for (int it = 0; it < 32; ++it) {
      const int m = it * 16 + wave * 4 + rl;
      w[it] = ldg16(w_q + ((size_t)m * 32 + h) * 128 + dl * 8);
    }
    for (int i = tid; i < 1024; i += 256) xs[i >> 9][i & 511] = bf2f(xB[i]);
    __syncthreads();
    float acc[2][8];
#pragma unroll
    for (int b = 0; b < 2; ++b)
#pragma unroll
      for (int k = 0; k < 8; ++k) acc[b][k] = 0.f;
#pragma unroll
    for (int it = 0; it < 32; ++it) {
      const int m = it * 16 + wave * 4 + rl;
      fma8(acc[0], xs[0][m], w[it]);
      fma8(acc[1], xs[1][m], w[it]);
    }
    u32x4 wk[8];
#pragma unroll
    for (int it = 0; it < 8; ++it) {
      const int c = cq * 128 + it * 16 + wave * 4 + rl;
      wk[it] = ldg16(w_uk + ((size_t)c * 32 + h) * 128 + dl * 8);
    }
#pragma unroll
    for (int b = 0; b < 2; ++b)
#pragma unroll
      for (int k = 0; k < 8; ++k) {
        float v = acc[b][k];
        v += shx(v, 16); v += shx(v, 32);
        acc[b][k] = v;
      }
    if (lane < 16) {
#pragma unroll
      for (int b = 0; b < 2; ++b)
#pragma unroll
        for (int k = 0; k < 8; ++k) red[wave][b][dl * 8 + k] = acc[b][k];
    }
    __syncthreads();
    {
      const int b = tid >> 7, d = tid & 127;
      qs[b][d] = red[0][b][d] + red[1][b][d] + red[2][b][d] + red[3][b][d];
    }
    __syncthreads();
    float qv[2][8];
#pragma unroll
    for (int b = 0; b < 2; ++b)
#pragma unroll
      for (int k = 0; k < 8; ++k) qv[b][k] = qs[b][dl * 8 + k];
#pragma unroll
    for (int it = 0; it < 8; ++it) {
      const int c = cq * 128 + it * 16 + wave * 4 + rl;
      float s[2] = {0.f, 0.f};
#pragma unroll
      for (int k = 0; k < 4; ++k) {
        const float w0 = bf_lo(wk[it][k]), w1 = bf_hi(wk[it][k]);
#pragma unroll
        for (int b = 0; b < 2; ++b) {
          s[b] = fmaf(w0, qv[b][2 * k], s[b]);
          s[b] = fmaf(w1, qv[b][2 * k + 1], s[b]);
        }
      }
#pragma unroll
      for (int b = 0; b < 2; ++b) {
        float v = s[b];
        v += shx(v, 1); v += shx(v, 2); v += shx(v, 4); v += shx(v, 8);
        if (dl == 0) qabs[((size_t)b * 32 + h) * 512 + c] = f2bf(v);
      }
    }
  }
}

constexpr int LDS_C = 0;
constexpr int LDS_P = 128 * 1024;
constexpr int LDS_ST = LDS_P + 32 * 256;
constexpr int LDS_K2 = LDS_ST + 2 * 4 * 32 * 4;

DI unsigned cswz(unsigned row) { return ((row & 3) << 2) | ((row >> 2) & 3); }
DI unsigned coff(unsigned row, unsigned ch) { return row * 1024u + 16u * (ch ^ cswz(row)); }
DI unsigned poff(unsigned head, unsigned ch) { return head * 256u + 16u * (ch ^ (head & 15)); }

__global__ __launch_bounds__(256) void k2_attn(const u16* __restrict__ kc, const u16* __restrict__ vc,
                                               const u16* __restrict__ ckv, const u16* __restrict__ qA,
                                               const u16* __restrict__ qabs, float* __restrict__ pao,
                                               float* __restrict__ paml, float* __restrict__ pbo,
                                               float* __restrict__ pbml) {
  __shared__ __attribute__((aligned(16))) char lds[LDS_K2];
  const int tid = threadIdx.x, lane = tid & 63;
  const int wave = __builtin_amdgcn_readfirstlane(tid >> 6);
  const int blk = blockIdx.x;
  if (blk < 128) {
    const int b = blk >> 6, g = (blk >> 3) & 7, s = blk & 7;
    const int r = lane >> 4, c = lane & 15;
    const size_t kbase = ((size_t)(b * HKV + g) * SK + (SK - WIN) + s * 256 + wave * 64) * DH + c * 8;
    u32x4 kk[16], vv[16];
#pragma unroll
    for (int i = 0; i < 16; ++i) kk[i] = ldg16nt(kc + kbase + (size_t)(4 * i + r) * DH);
#pragma unroll
    for (int i = 0; i < 16; ++i) vv[i] = ldg16nt(vc + kbase + (size_t)(4 * i + r) * DH);
    u32x4 q[4];
#pragma unroll
    for (int hh = 0; hh < 4; ++hh) q[hh] = ldg16(qA + b * 4096 + (g * 4 + hh) * 128 + c * 8);
    float sc[16][4];
#pragma unroll
    for (int i = 0; i < 16; ++i)
#pragma unroll
      for (int hh = 0; hh < 4; ++hh) {
        float d = 0.f;
#pragma unroll
        for (int k = 0; k < 4; ++k) d = dot2(kk[i][k], q[hh][k], d);
        d += shx(d, 1); d += shx(d, 2); d += shx(d, 4); d += shx(d, 8);
        sc[i][hh] = d * SCALE_LOG2;
      }
    float mx[4], ls[4], acc[4][8];
#pragma unroll
    for (int hh = 0; hh < 4; ++hh) {
      float m = sc[0][hh];
#pragma unroll
      for (int i = 1; i < 16; ++i) m = fmaxf(m, sc[i][hh]);
      m = fmaxf(m, shx(m, 16)); m = fmaxf(m, shx(m, 32));
      mx[hh] = m;
      float l = 0.f;
#pragma unroll
      for (int i = 0; i < 16; ++i) { sc[i][hh] = __builtin_amdgcn_exp2f(sc[i][hh] - m); l += sc[i][hh]; }
      l += shx(l, 16); l += shx(l, 32);
      ls[hh] = l;
#pragma unroll
      for (int k = 0; k < 8; ++k) acc[hh][k] = 0.f;
    }
#pragma unroll
    for (int i = 0; i < 16; ++i)
#pragma unroll
      for (int hh = 0; hh < 4; ++hh) fma8(acc[hh], sc[i][hh], vv[i]);
#pragma unroll
    for (int hh = 0; hh < 4; ++hh)
#pragma unroll
      for (int k = 0; k < 8; ++k) {
        float v = acc[hh][k];
        v += shx(v, 16); v += shx(v, 32);
        acc[hh][k] = v;
      }
    float* rec = reinterpret_cast<float*>(lds);
    if (lane < 16) {
#pragma unroll
      for (int hh = 0; hh < 4; ++hh) {
        float* p = rec + (wave * 4 + hh) * 132;
#pragma unroll
        for (int k = 0; k < 8; ++k) p[4 + c * 8 + k] = acc[hh][k];
        if (c == 0) { p[0] = mx[hh]; p[1] = ls[hh]; }
      }
    }
    __syncthreads();
    {
      const int hh = wave;
      float M = -INFINITY;
#pragma unroll
      for (int w = 0; w < 4; ++w) M = fmaxf(M, rec[(w * 4 + hh) * 132]);
      float L = 0.f, o0 = 0.f, o1 = 0.f;
#pragma unroll
      for (int w = 0; w < 4; ++w) {
        const float* p = rec + (w * 4 + hh) * 132;
        const float wt = __builtin_amdgcn_exp2f(p[0] - M);
        L = fmaf(wt, p[1], L);
        o0 = fmaf(wt, p[4 + 2 * lane], o0);
        o1 = fmaf(wt, p[4 + 2 * lane + 1], o1);
      }
      const int head = g * 4 + hh;
      const size_t slot = ((size_t)b * 32 + head) * SA + s;
      *reinterpret_cast<f32x2*>(pao + slot * 128 + 2 * lane) = f32x2{o0, o1};
      if (lane == 0) *reinterpret_cast<f32x2*>(paml + slot * 2) = f32x2{M, L};
    }
  } else {
    const int t = blk - 128, b = t >> 6, s = t & 63;
    const u16* cbase = ckv + ((size_t)b * SK + s * 128) * DC;
#pragma unroll
    for (int i = 0; i < 32; ++i) {
      const int row = wave * 32 + i;
      const u16* src = cbase + (size_t)row * DC + ((lane ^ cswz(row)) * 8);
      __builtin_amdgcn_global_load_lds((const unsigned*)src, (unsigned*)(lds + LDS_C + row * 1024), 16, 0, 0);
    }
    const int r = lane & 31, hf = lane >> 5;
    u32x4 qf[32];
#pragma unroll
    for (int ks = 0; ks < 32; ++ks) qf[ks] = ldg16(qabs + ((size_t)b * 32 + r) * 512 + ks * 16 + hf * 8);
    asm volatile("s_waitcnt vmcnt(0)" ::: "memory");
    __syncthreads();
    f32x16 st;
#pragma unroll
    for (int i = 0; i < 16; ++i) st[i] = 0.f;
#pragma unroll
    for (int ks = 0; ks < 32; ++ks) {
      const b16x8 a = *reinterpret_cast<const b16x8*>(lds + LDS_C + coff(wave * 32 + r, 2 * ks + hf));
      st = __builtin_amdgcn_mfma_f32_32x32x16_bf16(a, __builtin_bit_cast(b16x8, qf[ks]), st, 0, 0, 0);
    }
    float* stm = reinterpret_cast<float*>(lds + LDS_ST);
    float* stl = stm + 128;
    float m = st[0];
#pragma unroll
    for (int i = 1; i < 16; ++i) m = fmaxf(m, st[i]);
    m = fmaxf(m, shx(m, 32));
    if (hf == 0) stm[wave * 32 + r] = m;
    __syncthreads();
    const float M = fmaxf(fmaxf(stm[r], stm[32 + r]), fmaxf(stm[64 + r], stm[96 + r]));
    const float Ms = M * SCALE_LOG2;
    float l = 0.f;
#pragma unroll
    for (int i = 0; i < 16; ++i) { st[i] = __builtin_amdgcn_exp2f(st[i] * SCALE_LOG2 - Ms); l += st[i]; }
    l += shx(l, 32);
    if (hf == 0) stl[wave * 32 + r] = l;
#pragma unroll
    for (int gq = 0; gq < 4; ++gq) {
      const int key = wave * 32 + 8 * gq + 4 * hf;
      u32x2 pk = {pack_bf(st[4 * gq], st[4 * gq + 1]), pack_bf(st[4 * gq + 2], st[4 * gq + 3])};
      *reinterpret_cast<u32x2*>(lds + LDS_P + poff(r, key >> 3) + 8 * ((key >> 2) & 1)) = pk;
    }
    __syncthreads();
    f32x16 ot[4];
#pragma unroll
    for (int tt = 0; tt < 4; ++tt)
#pragma unroll
      for (int i = 0; i < 16; ++i) ot[tt][i] = 0.f;
    const int blk16 = (lane >> 4) & 1, qq = (lane & 15) >> 2, pp = lane & 3;
#pragma unroll
    for (int ks = 0; ks < 8; ++ks) {
      const b16x8 pf = *reinterpret_cast<const b16x8*>(lds + LDS_P + poff(r, 2 * ks + hf));
#pragma unroll
      for (int tt = 0; tt < 4; ++tt) {
        const unsigned ch = 16 * wave + 4 * tt + 2 * blk16 + (pp >> 1);
        const unsigned row0 = 16 * ks + 8 * hf + qq;
        const s16x4 lo = __builtin_amdgcn_ds_read_tr16_b64_v4i16(
            (__attribute__((address_space(3))) s16x4*)(lds + LDS_C + coff(row0, ch) + 8 * (pp & 1)));
        const s16x4 hi = __builtin_amdgcn_ds_read_tr16_b64_v4i16(
            (__attribute__((address_space(3))) s16x4*)(lds + LDS_C + coff(row0 + 4, ch) + 8 * (pp & 1)));
        const __attribute__((ext_vector_type(8))) short a8 = __builtin_shufflevector(lo, hi, 0, 1, 2, 3, 4, 5, 6, 7);
        ot[tt] = __builtin_amdgcn_mfma_f32_32x32x16_bf16(__builtin_bit_cast(b16x8, a8), pf, ot[tt], 0, 0, 0);
      }
    }
    float* po = pbo + (((size_t)b * SB + s) * 32 + r) * 512;
#pragma unroll
    for (int tt = 0; tt < 4; ++tt)
#pragma unroll
      for (int gq = 0; gq < 4; ++gq) {
        const int c = wave * 128 + tt * 32 + 8 * gq + 4 * hf;
        *reinterpret_cast<f32x4*>(po + c) = f32x4{ot[tt][4 * gq], ot[tt][4 * gq + 1], ot[tt][4 * gq + 2], ot[tt][4 * gq + 3]};
      }
    if (wave == 0 && hf == 0) {
      const float L = (stl[r] + stl[32 + r]) + (stl[64 + r] + stl[96 + r]);
      *reinterpret_cast<f32x2*>(pbml + (((size_t)b * SB + s) * 32 + r) * 2) = f32x2{Ms, L};
    }
  }
}

__global__ __launch_bounds__(256) void k3_out(const u16* __restrict__ wo, const u16* __restrict__ w_uv,
                                              const u16* __restrict__ w_o, const float* __restrict__ pao,
                                              const float* __restrict__ paml, const float* __restrict__ pbo,
                                              const float* __restrict__ pbml, float* __restrict__ sla,
                                              float* __restrict__ slb) {
  __shared__ float os[2][128];
  __shared__ float wts[2][64];
  __shared__ float red[4][2][512];
  const int tid = threadIdx.x, lane = tid & 63, wave = tid >> 6;
  const int blk = blockIdx.x;
  const int dl = lane & 15, rl = lane >> 4;
  if (blk < 128) {
    const int h = blk >> 2, cg = blk & 3;
    u32x4 w[8];
#pragma unroll
    for (int it = 0; it < 8; ++it) {
      const int d = it * 16 + wave * 4 + rl;
      w[it] = ldg16(wo + ((size_t)h * 128 + d) * 512 + cg * 128 + dl * 8);
    }
    {
      const int b = tid >> 7, d = tid & 127;
      const float* ml = paml + ((size_t)b * 32 + h) * SA * 2;
      float M = -INFINITY;
#pragma unroll
      for (int s = 0; s < SA; ++s) M = fmaxf(M, ml[2 * s]);
      float L = 0.f, o = 0.f;
#pragma unroll
      for (int s = 0; s < SA; ++s) {
        const float wt = __builtin_amdgcn_exp2f(ml[2 * s] - M);
        L = fmaf(wt, ml[2 * s + 1], L);
        o = fmaf(wt, pao[(((size_t)b * 32 + h) * SA + s) * 128 + d], o);
      }
      os[b][d] = o / L;
    }
    __syncthreads();
    float acc[2][8];
#pragma unroll
    for (int b = 0; b < 2; ++b)
#pragma unroll
      for (int k = 0; k < 8; ++k) acc[b][k] = 0.f;
#pragma unroll
    for (int it = 0; it < 8; ++it) {
      const int d = it * 16 + wave * 4 + rl;
      fma8(acc[0], os[0][d], w[it]);
      fma8(acc[1], os[1][d], w[it]);
    }
#pragma unroll
    for (int b = 0; b < 2; ++b)
#pragma unroll
      for (int k = 0; k < 8; ++k) {
        float v = acc[b][k];
        v += shx(v, 16); v += shx(v, 32);
        acc[b][k] = v;
      }
    if (lane < 16) {
#pragma unroll
      for (int b = 0; b < 2; ++b)
#pragma unroll
        for (int k = 0; k < 8; ++k) red[wave][b][dl * 8 + k] = acc[b][k];
    }
    __syncthreads();
    {
      const int b = tid >> 7, c = tid & 127;
      sla[((size_t)blk * 2 + b) * 128 + c] = (red[0][b][c] + red[1][b][c]) + (red[2][b][c] + red[3][b][c]);
    }
  } else {
    const int j = blk - 128, h = j & 31, cq = j >> 5;
    u32x4 wv[8];
#pragma unroll
    for (int it = 0; it < 8; ++it) {
      const int c = cq * 128 + it * 16 + wave * 4 + rl;
      wv[it] = ldg16(w_uv + ((size_t)c * 32 + h) * 128 + dl * 8);
    }
    if (tid < 128) {
      const int b = tid >> 6, s = tid & 63;
      const float* ml = pbml + (((size_t)b * SB + s) * 32 + h) * 2;
      const float m = ml[0], l = ml[1];
      float M = m;
      M = fmaxf(M, shx(M, 1)); M = fmaxf(M, shx(M, 2)); M = fmaxf(M, shx(M, 4));
      M = fmaxf(M, shx(M, 8)); M = fmaxf(M, shx(M, 16)); M = fmaxf(M, shx(M, 32));
      const float wt = __builtin_amdgcn_exp2f(m - M);
      float L = wt * l;
      L += shx(L, 1); L += shx(L, 2); L += shx(L, 4); L += shx(L, 8); L += shx(L, 16); L += shx(L, 32);
      wts[b][s] = wt / L;
    }
    __syncthreads();
    {
      const int b = tid >> 7, c = tid & 127;
      const float* p = pbo + ((size_t)b * SB * 32 + h) * 512 + cq * 128 + c;
      float o = 0.f;
#pragma unroll 16
      for (int s = 0; s < SB; ++s) o = fmaf(wts[b][s], p[(size_t)s * 32 * 512], o);
      os[b][c] = o;
    }
    __syncthreads();
    float acc[2][8];
#pragma unroll
    for (int b = 0; b < 2; ++b)
#pragma unroll
      for (int k = 0; k < 8; ++k) acc[b][k] = 0.f;
#pragma unroll
    for (int it = 0; it < 8; ++it) {
      const int c = it * 16 + wave * 4 + rl;
      fma8(acc[0], os[0][c], wv[it]);
      fma8(acc[1], os[1][c], wv[it]);
    }
#pragma unroll
    for (int b = 0; b < 2; ++b)
#pragma unroll
      for (int k = 0; k < 8; ++k) {
        float v = acc[b][k];
        v += shx(v, 16); v += shx(v, 32);
        acc[b][k] = v;
      }
    if (lane < 16) {
#pragma unroll
      for (int b = 0; b < 2; ++b)
#pragma unroll
        for (int k = 0; k < 8; ++k) red[wave][b][dl * 8 + k] = acc[b][k];
    }
    __syncthreads();
    {
      const int b = tid >> 7, d = tid & 127;
      os[b][d] = (red[0][b][d] + red[1][b][d]) + (red[2][b][d] + red[3][b][d]);
    }
    __syncthreads();
    float ac2[2][8];
#pragma unroll
    for (int b = 0; b < 2; ++b)
#pragma unroll
      for (int k = 0; k < 8; ++k) ac2[b][k] = 0.f;
#pragma unroll 8
    for (int it = 0; it < 32; ++it) {
      const int d = it * 4 + wave;
      const u32x4 w = ldg16(w_o + ((size_t)h * 128 + d) * 512 + lane * 8);
      fma8(ac2[0], os[0][d], w);
      fma8(ac2[1], os[1][d], w);
    }
    __syncthreads();
#pragma unroll
    for (int b = 0; b < 2; ++b)
#pragma unroll
      for (int k = 0; k < 8; ++k) red[wave][b][lane * 8 + k] = ac2[b][k];
    __syncthreads();
#pragma unroll
    for (int i = 0; i < 4; ++i) {
      const int idx = tid + 256 * i, b = idx >> 9, m = idx & 511;
      slb[((size_t)j * 2 + b) * 512 + m] = (red[0][b][m] + red[1][b][m]) + (red[2][b][m] + red[3][b][m]);
    }
  }
}

__global__ __launch_bounds__(256) void k4_final(const float* __restrict__ sla, const float* __restrict__ slb,
                                                u16* __restrict__ out) {
  const int gid = blockIdx.x * 256 + threadIdx.x;
  const int o = gid >> 3, part = gid & 7;
  const int b = o >> 10, col = o & 1023;
  float v = 0.f;
  if (col < 512) {
    const int cg = col >> 7, c = col & 127;
#pragma unroll
    for (int i = 0; i < 4; ++i) {
      const int h = part * 4 + i;
      v += sla[((size_t)(h * 4 + cg) * 2 + b) * 128 + c];
    }
  } else {
    const int m = col - 512;
#pragma unroll
    for (int i = 0; i < 16; ++i) v += slb[((size_t)(part * 16 + i) * 2 + b) * 512 + m];
  }
  v += __shfl_xor(v, 1, 64); v += __shfl_xor(v, 2, 64); v += __shfl_xor(v, 4, 64);
  if (part == 0) out[o] = f2bf(v);
}

extern "C" void kernel_launch(void* const* d_in, const int* in_sizes, int n_in, void* d_out, int out_size,
                              void* d_ws, size_t ws_size, hipStream_t stream) {
  const u16* A_x = (const u16*)d_in[0];
  const u16* A_k = (const u16*)d_in[1];
  const u16* A_v = (const u16*)d_in[2];
  const u16* A_wq = (const u16*)d_in[3];
  const u16* A_wo = (const u16*)d_in[4];
  const u16* B_x = (const u16*)d_in[5];
  const u16* B_ckv = (const u16*)d_in[6];
  const u16* B_wq = (const u16*)d_in[7];
  const u16* B_wuk = (const u16*)d_in[8];
  const u16* B_wuv = (const u16*)d_in[9];
  const u16* B_wo = (const u16*)d_in[10];
  char* ws = (char*)d_ws;
  u16* qA = (u16*)(ws + WS_QA);
  u16* qabs = (u16*)(ws + WS_QABS);
  float* pao = (float*)(ws + WS_PAO);
  float* paml = (float*)(ws + WS_PAML);
  float* pbo = (float*)(ws + WS_PBO);
  float* pbml = (float*)(ws + WS_PBML);
  float* sla = (float*)(ws + WS_SLA);
  float* slb = (float*)(ws + WS_SLB);
  k1_qproj<<<192, 256, 0, stream>>>(A_x, A_wq, B_x, B_wq, B_wuk, qA, qabs);
  k2_attn<<<256, 256, 0, stream>>>(A_k, A_v, B_ckv, qA, qabs, pao, paml, pbo, pbml);
  k3_out<<<256, 256, 0, stream>>>(A_wo, B_wuv, B_wo, pao, paml, pbo, pbml, sla, slb);
  k4_final<<<64, 256, 0, stream>>>(sla, slb, (u16*)d_out);
}
```

```cpp
#include <hip/hip_runtime.h>
#include <hip/hip_bf16.h>
#include <stdint.h>

typedef __attribute__((ext_vector_type(4))) unsigned u32x4;
typedef __attribute__((ext_vector_type(2))) unsigned u32x2;
typedef __attribute__((ext_vector_type(4))) float f32x4;
typedef __attribute__((ext_vector_type(2))) float f32x2;
typedef __attribute__((ext_vector_type(16))) float f32x16;
typedef __attribute__((ext_vector_type(8))) __bf16 b16x8;
typedef __attribute__((ext_vector_type(2))) __bf16 b16x2;
typedef __attribute__((ext_vector_type(4))) short s16x4;
typedef unsigned short u16;

#define DI __device__ __forceinline__

constexpr int NB = 2;
constexpr int DM = 512;
constexpr int HQ = 32, HKV = 8, SK = 8192, DH = 128, WIN = 2048;
constexpr int DC = 512;
constexpr int SA = 8;
constexpr int SB = 64;
constexpr float SCALE_LOG2 = 0.08838834764831845f * 1.4426950408889634f;

constexpr size_t WS_QA   = 0;
constexpr size_t WS_QABS = WS_QA + 2 * 4096 * 2;
constexpr size_t WS_PAO  = WS_QABS + 2 * 32 * 512 * 2;
constexpr size_t WS_PAML = WS_PAO + (size_t)2 * 32 * SA * 128 * 4;
constexpr size_t WS_PBO  = WS_PAML + (size_t)2 * 32 * SA * 2 * 4;
constexpr size_t WS_PBML = WS_PBO + (size_t)2 * SB * 32 * 512 * 4;
constexpr size_t WS_SLA  = WS_PBML + (size_t)2 * SB * 32 * 2 * 4;
constexpr size_t WS_SLB  = WS_SLA + (size_t)128 * 2 * 128 * 4;
constexpr size_t WS_END  = WS_SLB + (size_t)128 * 2 * 512 * 4;

#define STAMP_BEGIN(k)
#define STAMP_END(k)
DI float bf_lo(unsigned u) { return __builtin_bit_cast(float, u << 16); }
DI float bf_hi(unsigned u) { return __builtin_bit_cast(float, u & 0xffff0000u); }
DI float bf2f(u16 v) { return __builtin_bit_cast(float, (unsigned)v << 16); }
DI u16 f2bf(float f) { return __builtin_bit_cast(u16, __float2bfloat16(f)); }
DI unsigned pack_bf(float lo, float hi) { return (unsigned)f2bf(lo) | ((unsigned)f2bf(hi) << 16); }
DI float dot2(unsigned a, unsigned b, float acc) {
  return __builtin_amdgcn_fdot2_f32_bf16(__builtin_bit_cast(b16x2, a), __builtin_bit_cast(b16x2, b), acc, false);
}
DI u32x4 ldg16(const void* p) { return *reinterpret_cast<const u32x4*>(p); }
DI u32x4 ldg16nt(const void* p) { return __builtin_nontemporal_load(reinterpret_cast<const u32x4*>(p)); }
template <int CTRL> DI float dppf(float x) {
  return __builtin_bit_cast(float, __builtin_amdgcn_mov_dpp(__builtin_bit_cast(int, x), CTRL, 0xf, 0xf, true));
}
DI float row16_sum(float x) {
  x += dppf<0x128>(x); x += dppf<0x124>(x); x += dppf<0x122>(x); x += dppf<0x121>(x);
  return x;
}
DI float row16_max(float x) {
  x = fmaxf(x, dppf<0x128>(x)); x = fmaxf(x, dppf<0x124>(x)); x = fmaxf(x, dppf<0x122>(x)); x = fmaxf(x, dppf<0x121>(x));
  return x;
}
DI float xor8_sum(float x) { return x + dppf<0x128>(x); }
DI float xrow_sum(float x) {
  auto s = __builtin_amdgcn_permlane16_swap(__float_as_uint(x), __float_as_uint(x), false, false);
  x = __uint_as_float(s[0]) + __uint_as_float(s[1]);
  auto t = __builtin_amdgcn_permlane32_swap(__float_as_uint(x), __float_as_uint(x), false, false);
  return __uint_as_float(t[0]) + __uint_as_float(t[1]);
}
DI float xrow_max(float x) {
  auto s = __builtin_amdgcn_permlane16_swap(__float_as_uint(x), __float_as_uint(x), false, false);
  x = fmaxf(__uint_as_float(s[0]), __uint_as_float(s[1]));
  auto t = __builtin_amdgcn_permlane32_swap(__float_as_uint(x), __float_as_uint(x), false, false);
  return fmaxf(__uint_as_float(t[0]), __uint_as_float(t[1]));
}
DI float half_sum(float x) {
  auto t = __builtin_amdgcn_permlane32_swap(__float_as_uint(x), __float_as_uint(x), false, false);
  return __uint_as_float(t[0]) + __uint_as_float(t[1]);
}
DI float half_max(float x) {
  auto t = __builtin_amdgcn_permlane32_swap(__float_as_uint(x), __float_as_uint(x), false, false);
  return fmaxf(__uint_as_float(t[0]), __uint_as_float(t[1]));
}

DI void fma8(float* acc, float s, const u32x4& w) {
#pragma unroll
  for (int k = 0; k < 4; ++k) {
    acc[2 * k]     = fmaf(s, bf_lo(w[k]), acc[2 * k]);
    acc[2 * k + 1] = fmaf(s, bf_hi(w[k]), acc[2 * k + 1]);
  }
}

__global__ __launch_bounds__(256) void k1_qproj(const u16* __restrict__ xA, const u16* __restrict__ wq,
                                                const u16* __restrict__ xB, const u16* __restrict__ w_q,
                                                const u16* __restrict__ w_uk, u16* __restrict__ qA,
                                                u16* __restrict__ qabs) {
  __shared__ float xs[2][512];
  __shared__ float red[4][2][128];
  __shared__ float qs[2][128];
  STAMP_BEGIN(0)
  const int tid = threadIdx.x, lane = tid & 63, wave = tid >> 6;
  const int blk = blockIdx.x;
  if (blk < 64) {
    const int n0 = blk * 64, cg = lane & 7, rl = lane >> 3;
    u32x4 w[16];
#pragma unroll
    for (int it = 0; it < 16; ++it) {
      const int row = it * 32 + wave * 8 + rl;
      w[it] = ldg16nt(wq + (size_t)row * 4096 + n0 + cg * 8);
    }
    for (int i = tid; i < 1024; i += 256) xs[i >> 9][i & 511] = bf2f(xA[i]);
    __syncthreads();
    float acc[2][8];
#pragma unroll
    for (int b = 0; b < 2; ++b)
#pragma unroll
      for (int k = 0; k < 8; ++k) acc[b][k] = 0.f;
#pragma unroll
    for (int it = 0; it < 16; ++it) {
      const int row = it * 32 + wave * 8 + rl;
      fma8(acc[0], xs[0][row], w[it]);
      fma8(acc[1], xs[1][row], w[it]);
    }
#pragma unroll
    for (int b = 0; b < 2; ++b)
#pragma unroll
      for (int k = 0; k < 8; ++k) {
        float v = acc[b][k];
        v = xrow_sum(xor8_sum(v));
        acc[b][k] = v;
      }
    if (lane < 8) {
#pragma unroll
      for (int b = 0; b < 2; ++b)
#pragma unroll
        for (int k = 0; k < 8; ++k) red[wave][b][cg * 8 + k] = acc[b][k];
    }
    __syncthreads();
    if (tid < 128) {
      const int b = tid >> 6, c = tid & 63;
      const float v = red[0][b][c] + red[1][b][c] + red[2][b][c] + red[3][b][c];
      qA[b * 4096 + n0 + c] = f2bf(v);
    }
  } else {
    const int j = blk - 64, h = j & 31, cq = j >> 5;
    const int dl = lane & 15, rl = lane >> 4;
    u32x4 w[32];
#pragma unroll
    for (int it = 0; it < 32; ++it) {
      const int m = it * 16 + wave * 4 + rl;
      w[it] = ldg16nt(w_q + ((size_t)m * 32 + h) * 128 + dl * 8);
    }
    for (int i = tid; i < 1024; i += 256) xs[i >> 9][i & 511] = bf2f(xB[i]);
    __syncthreads();
    float acc[2][8];
#pragma unroll
    for (int b = 0; b < 2; ++b)
#pragma unroll
      for (int k = 0; k < 8; ++k) acc[b][k] = 0.f;
#pragma unroll
    for (int it = 0; it < 32; ++it) {
      const int m = it * 16 + wave * 4 + rl;
      fma8(acc[0], xs[0][m], w[it]);
      fma8(acc[1], xs[1][m], w[it]);
    }
    u32x4 wk[8];
#pragma unroll
    for (int it = 0; it < 8; ++it) {
      const int c = cq * 128 + it * 16 + wave * 4 + rl;
      wk[it] = ldg16nt(w_uk + ((size_t)c * 32 + h) * 128 + dl * 8);
    }
#pragma unroll
    for (int b = 0; b < 2; ++b)
#pragma unroll
      for (int k = 0; k < 8; ++k) {
        float v = acc[b][k];
        v = xrow_sum(v);
        acc[b][k] = v;
      }
    if (lane < 16) {
#pragma unroll
      for (int b = 0; b < 2; ++b)
#pragma unroll
        for (int k = 0; k < 8; ++k) red[wave][b][dl * 8 + k] = acc[b][k];
    }
    __syncthreads();
    {
      const int b = tid >> 7, d = tid & 127;
      qs[b][d] = red[0][b][d] + red[1][b][d] + red[2][b][d] + red[3][b][d];
    }
    __syncthreads();
    float qv[2][8];
#pragma unroll
    for (int b = 0; b < 2; ++b)
#pragma unroll
      for (int k = 0; k < 8; ++k) qv[b][k] = qs[b][dl * 8 + k];
#pragma unroll
    for (int it = 0; it < 8; ++it) {
      const int c = cq * 128 + it * 16 + wave * 4 + rl;
      float s[2] = {0.f, 0.f};
#pragma unroll
      for (int k = 0; k < 4; ++k) {
        const float w0 = bf_lo(wk[it][k]), w1 = bf_hi(wk[it][k]);
#pragma unroll
        for (int b = 0; b < 2; ++b) {
          s[b] = fmaf(w0, qv[b][2 * k], s[b]);
          s[b] = fmaf(w1, qv[b][2 * k + 1], s[b]);
        }
      }
#pragma unroll
      for (int b = 0; b < 2; ++b) {
        float v = s[b];
        v = row16_sum(v);
        if (dl == 0) qabs[((size_t)b * 32 + h) * 512 + c] = f2bf(v);
      }
    }
  }
  STAMP_END(0)
}

constexpr int LDS_C = 0;
constexpr int LDS_Q = 128 * 1024;
constexpr int LDS_P = LDS_Q;
constexpr int LDS_ST = LDS_Q + 32 * 256;
constexpr int LDS_K2 = LDS_Q + 32 * 1024;

DI unsigned cswz(unsigned row) { return ((row & 3) << 2) | ((row >> 2) & 3); }
DI unsigned coff(unsigned row, unsigned ch) { return row * 1024u + 16u * (ch ^ cswz(row)); }
DI unsigned poff(unsigned head, unsigned ch) { return head * 256u + 16u * (ch ^ (head & 15)); }

__global__ __launch_bounds__(256) void k2_attn(const u16* __restrict__ kc, const u16* __restrict__ vc,
                                               const u16* __restrict__ ckv, const u16* __restrict__ qA,
                                               const u16* __restrict__ qabs, float* __restrict__ pao,
                                               float* __restrict__ paml, float* __restrict__ pbo,
                                               float* __restrict__ pbml) {
  __shared__ __attribute__((aligned(16))) char lds[LDS_K2];
  STAMP_BEGIN(1)
  const int tid = threadIdx.x, lane = tid & 63;
  const int wave = __builtin_amdgcn_readfirstlane(tid >> 6);
  const int blk = blockIdx.x;
  if (blk < 128) {
    const int b = blk >> 6, g = (blk >> 3) & 7, s = blk & 7;
    const int r = lane >> 4, c = lane & 15;
    const size_t kbase = ((size_t)(b * HKV + g) * SK + (SK - WIN) + s * 256 + wave * 64) * DH + c * 8;
    u32x4 kk[16], vv[16];
#pragma unroll
    for (int i = 0; i < 16; ++i) kk[i] = ldg16nt(kc + kbase + (size_t)(4 * i + r) * DH);
#pragma unroll
    for (int i = 0; i < 16; ++i) vv[i] = ldg16nt(vc + kbase + (size_t)(4 * i + r) * DH);
    u32x4 q[4];
#pragma unroll
    for (int hh = 0; hh < 4; ++hh) q[hh] = ldg16(qA + b * 4096 + (g * 4 + hh) * 128 + c * 8);
    float sc[16][4];
#pragma unroll
    for (int i = 0; i < 16; ++i)
#pragma unroll
      for (int hh = 0; hh < 4; ++hh) {
        float d = 0.f;
#pragma unroll
        for (int k = 0; k < 4; ++k) d = dot2(kk[i][k], q[hh][k], d);
        d = row16_sum(d);
        sc[i][hh] = d * SCALE_LOG2;
      }
    float mx[4], ls[4], acc[4][8];
#pragma unroll
    for (int hh = 0; hh < 4; ++hh) {
      float m = sc[0][hh];
#pragma unroll
      for (int i = 1; i < 16; ++i) m = fmaxf(m, sc[i][hh]);
      m = xrow_max(m);
      mx[hh] = m;
      float l = 0.f;
#pragma unroll
      for (int i = 0; i < 16; ++i) { sc[i][hh] = __builtin_amdgcn_exp2f(sc[i][hh] - m); l += sc[i][hh]; }
      l = xrow_sum(l);
      ls[hh] = l;
#pragma unroll
      for (int k = 0; k < 8; ++k) acc[hh][k] = 0.f;
    }
#pragma unroll
    for (int i = 0; i < 16; ++i)
#pragma unroll
      for (int hh = 0; hh < 4; ++hh) fma8(acc[hh], sc[i][hh], vv[i]);
#pragma unroll
    for (int hh = 0; hh < 4; ++hh)
#pragma unroll
      for (int k = 0; k < 8; ++k) {
        float v = acc[hh][k];
        v = xrow_sum(v);
        acc[hh][k] = v;
      }
    float* rec = reinterpret_cast<float*>(lds);
    if (lane < 16) {
#pragma unroll
      for (int hh = 0; hh < 4; ++hh) {
        float* p = rec + (wave * 4 + hh) * 132;
#pragma unroll
        for (int k = 0; k < 8; ++k) p[4 + c * 8 + k] = acc[hh][k];
        if (c == 0) { p[0] = mx[hh]; p[1] = ls[hh]; }
      }
    }
    __syncthreads();
    {
      const int hh = wave;
      float M = -INFINITY;
#pragma unroll
      for (int w = 0; w < 4; ++w) M = fmaxf(M, rec[(w * 4 + hh) * 132]);
      float L = 0.f, o0 = 0.f, o1 = 0.f;
#pragma unroll
      for (int w = 0; w < 4; ++w) {
        const float* p = rec + (w * 4 + hh) * 132;
        const float wt = __builtin_amdgcn_exp2f(p[0] - M);
        L = fmaf(wt, p[1], L);
        o0 = fmaf(wt, p[4 + 2 * lane], o0);
        o1 = fmaf(wt, p[4 + 2 * lane + 1], o1);
      }
      const int head = g * 4 + hh;
      const size_t slot = ((size_t)b * 32 + head) * SA + s;
      *reinterpret_cast<f32x2*>(pao + slot * 128 + 2 * lane) = f32x2{o0, o1};
      if (lane == 0) *reinterpret_cast<f32x2*>(paml + slot * 2) = f32x2{M, L};
    }
  } else {
    const int t = blk - 128, b = t >> 6, s = t & 63;
    const u16* cbase = ckv + ((size_t)b * SK + s * 128) * DC;
#pragma unroll
    for (int i = 0; i < 32; ++i) {
      const int row = wave * 32 + i;
      const u16* src = cbase + (size_t)row * DC + ((lane ^ cswz(row)) * 8);
      __builtin_amdgcn_global_load_lds((const unsigned*)src, (unsigned*)(lds + LDS_C + row * 1024), 16, 0, 2);
    }
#pragma unroll
    for (int i = 0; i < 8; ++i) {
      const int row = wave * 8 + i;
      const u16* src = qabs + ((size_t)b * 32 + row) * DC + ((lane ^ cswz(row)) * 8);
      __builtin_amdgcn_global_load_lds((const unsigned*)src, (unsigned*)(lds + LDS_Q + row * 1024), 16, 0, 0);
    }
    const int r = lane & 31, hf = lane >> 5;
    asm volatile("s_waitcnt vmcnt(0)" ::: "memory");
    __syncthreads();
    f32x16 st;
#pragma unroll
    for (int i = 0; i < 16; ++i) st[i] = 0.f;
#pragma unroll
    for (int kh = 0; kh < 4; ++kh) {
      b16x8 af[8], qf[8];
#pragma unroll
      for (int j = 0; j < 8; ++j) {
        af[j] = *reinterpret_cast<const b16x8*>(lds + LDS_C + coff(wave * 32 + r, 2 * (kh * 8 + j) + hf));
        qf[j] = *reinterpret_cast<const b16x8*>(lds + LDS_Q + coff(r, 2 * (kh * 8 + j) + hf));
      }
      __builtin_amdgcn_sched_barrier(0);
#pragma unroll
      for (int j = 0; j < 8; ++j)
        st = __builtin_amdgcn_mfma_f32_32x32x16_bf16(af[j], qf[j], st, 0, 0, 0);
      __builtin_amdgcn_sched_barrier(0);
    }
    float* stm = reinterpret_cast<float*>(lds + LDS_ST);
    float* stl = stm + 128;
    float m = st[0];
#pragma unroll
    for (int i = 1; i < 16; ++i) m = fmaxf(m, st[i]);
    m = half_max(m);
    __syncthreads();
    if (hf == 0) stm[wave * 32 + r] = m;
    __syncthreads();
    const float M = fmaxf(fmaxf(stm[r], stm[32 + r]), fmaxf(stm[64 + r], stm[96 + r]));
    const float Ms = M * SCALE_LOG2;
    float l = 0.f;
#pragma unroll
    for (int i = 0; i < 16; ++i) { st[i] = __builtin_amdgcn_exp2f(st[i] * SCALE_LOG2 - Ms); l += st[i]; }
    l = half_sum(l);
    if (hf == 0) stl[wave * 32 + r] = l;
#pragma unroll
    for (int gq = 0; gq < 4; ++gq) {
      const int key = wave * 32 + 8 * gq + 4 * hf;
      u32x2 pk = {pack_bf(st[4 * gq], st[4 * gq + 1]), pack_bf(st[4 * gq + 2], st[4 * gq + 3])};
      *reinterpret_cast<u32x2*>(lds + LDS_P + poff(r, key >> 3) + 8 * ((key >> 2) & 1)) = pk;
    }
    __syncthreads();
    f32x16 ot[4];
#pragma unroll
    for (int tt = 0; tt < 4; ++tt)
#pragma unroll
      for (int i = 0; i < 16; ++i) ot[tt][i] = 0.f;
    const int blk16 = (lane >> 4) & 1, qq = (lane & 15) >> 2, pp = lane & 3;
    b16x8 pf[2];
    s16x4 lo[2][4], hi[2][4];
#define PV_LOAD(ks, buf)                                                                                      \
    {                                                                                                         \
      pf[buf] = *reinterpret_cast<const b16x8*>(lds + LDS_P + poff(r, 2 * (ks) + hf));                          \
      _Pragma("unroll") for (int tt = 0; tt < 4; ++tt) {                                                      \
        const unsigned ch = 16 * wave + 4 * tt + 2 * blk16 + (pp >> 1);                                       \
        const unsigned row0 = 16 * (ks) + 8 * hf + qq;                                                        \
        lo[buf][tt] = __builtin_amdgcn_ds_read_tr16_b64_v4i16(                                                \
            (__attribute__((address_space(3))) s16x4*)(lds + LDS_C + coff(row0, ch) + 8 * (pp & 1)));         \
        hi[buf][tt] = __builtin_amdgcn_ds_read_tr16_b64_v4i16(                                                \
            (__attribute__((address_space(3))) s16x4*)(lds + LDS_C + coff(row0 + 4, ch) + 8 * (pp & 1)));     \
      }                                                                                                       \
    }
    PV_LOAD(0, 0)
#pragma unroll
    for (int ks = 0; ks < 8; ++ks) {
      if (ks + 1 < 8) PV_LOAD(ks + 1, (ks + 1) & 1)
      __builtin_amdgcn_sched_barrier(0);
#pragma unroll
      for (int tt = 0; tt < 4; ++tt) {
        const __attribute__((ext_vector_type(8))) short a8 =
            __builtin_shufflevector(lo[ks & 1][tt], hi[ks & 1][tt], 0, 1, 2, 3, 4, 5, 6, 7);
        ot[tt] = __builtin_amdgcn_mfma_f32_32x32x16_bf16(__builtin_bit_cast(b16x8, a8), pf[ks & 1], ot[tt], 0, 0, 0);
      }
      __builtin_amdgcn_sched_barrier(0);
    }
#undef PV_LOAD
    __syncthreads();
#pragma unroll
    for (int tt = 0; tt < 4; ++tt)
#pragma unroll
      for (int gq = 0; gq < 4; ++gq) {
        const int c = wave * 128 + tt * 32 + 8 * gq + 4 * hf;
        *reinterpret_cast<f32x4*>(lds + LDS_C + r * 2064 + c * 4) =
            f32x4{ot[tt][4 * gq], ot[tt][4 * gq + 1], ot[tt][4 * gq + 2], ot[tt][4 * gq + 3]};
      }
    __syncthreads();
    float* po = pbo + ((size_t)b * SB + s) * 32 * 512;
#pragma unroll
    for (int i = 0; i < 16; ++i) {
      const int hd = wave * 8 + (i >> 1), c = (i & 1) * 256 + lane * 4;
      const f32x4 v = *reinterpret_cast<const f32x4*>(lds + LDS_C + hd * 2064 + c * 4);
      *reinterpret_cast<f32x4*>(po + hd * 512 + c) = v;
    }
    if (wave == 0 && hf == 0) {
      const float L = (stl[r] + stl[32 + r]) + (stl[64 + r] + stl[96 + r]);
      *reinterpret_cast<f32x2*>(pbml + (((size_t)b * SB + s) * 32 + r) * 2) = f32x2{Ms, L};
    }
  }
  STAMP_END(1)
}

__global__ __launch_bounds__(256) void k3_out(const u16* __restrict__ wo, const u16* __restrict__ w_uv,
                                              const u16* __restrict__ w_o, const float* __restrict__ pao,
                                              const float* __restrict__ paml, const float* __restrict__ pbo,
                                              const float* __restrict__ pbml, float* __restrict__ sla,
                                              float* __restrict__ slb) {
  __shared__ float os[2][128];
  __shared__ float wts[2][64];
  __shared__ float red[4][2][512];
  STAMP_BEGIN(2)
  const int tid = threadIdx.x, lane = tid & 63, wave = tid >> 6;
  const int blk = blockIdx.x;
  const int dl = lane & 15, rl = lane >> 4;
  if (blk < 128) {
    const int h = blk >> 2, cg = blk & 3;
    u32x4 w[8];
#pragma unroll
    for (int it = 0; it < 8; ++it) {
      const int d = it * 16 + wave * 4 + rl;
      w[it] = ldg16nt(wo + ((size_t)h * 128 + d) * 512 + cg * 128 + dl * 8);
    }
    {
      const int b = tid >> 7, d = tid & 127;
      const float* ml = paml + ((size_t)b * 32 + h) * SA * 2;
      float M = -INFINITY;
#pragma unroll
      for (int s = 0; s < SA; ++s) M = fmaxf(M, ml[2 * s]);
      float L = 0.f, o = 0.f;
#pragma unroll
      for (int s = 0; s < SA; ++s) {
        const float wt = __builtin_amdgcn_exp2f(ml[2 * s] - M);
        L = fmaf(wt, ml[2 * s + 1], L);
        o = fmaf(wt, pao[(((size_t)b * 32 + h) * SA + s) * 128 + d], o);
      }
      os[b][d] = o / L;
    }
    __syncthreads();
    float acc[2][8];
#pragma unroll
    for (int b = 0; b < 2; ++b)
#pragma unroll
      for (int k = 0; k < 8; ++k) acc[b][k] = 0.f;
#pragma unroll
    for (int it = 0; it < 8; ++it) {
      const int d = it * 16 + wave * 4 + rl;
      fma8(acc[0], os[0][d], w[it]);
      fma8(acc[1], os[1][d], w[it]);
    }
#pragma unroll
    for (int b = 0; b < 2; ++b)
#pragma unroll
      for (int k = 0; k < 8; ++k) {
        float v = acc[b][k];
        v = xrow_sum(v);
        acc[b][k] = v;
      }
    if (lane < 16) {
#pragma unroll
      for (int b = 0; b < 2; ++b)
#pragma unroll
        for (int k = 0; k < 8; ++k) red[wave][b][dl * 8 + k] = acc[b][k];
    }
    __syncthreads();
    {
      const int b = tid >> 7, c = tid & 127;
      sla[((size_t)blk * 2 + b) * 128 + c] = (red[0][b][c] + red[1][b][c]) + (red[2][b][c] + red[3][b][c]);
    }
  } else {
    const int j = blk - 128, h = j & 31, cq = j >> 5;
    float ml0 = 0.f, ml1 = 0.f;
    if (tid < 128) {
      const float* ml = pbml + (((size_t)(tid >> 6) * SB + (tid & 63)) * 32 + h) * 2;
      ml0 = ml[0]; ml1 = ml[1];
    }
    float pv[SB];
    {
      const float* p = pbo + ((size_t)(tid >> 7) * SB * 32 + h) * 512 + cq * 128 + (tid & 127);
#pragma unroll
      for (int s = 0; s < SB; ++s) pv[s] = __builtin_nontemporal_load(p + (size_t)s * 32 * 512);
    }
    u32x4 wv[8];
#pragma unroll
    for (int it = 0; it < 8; ++it) {
      const int c = cq * 128 + it * 16 + wave * 4 + rl;
      wv[it] = ldg16nt(w_uv + ((size_t)c * 32 + h) * 128 + dl * 8);
    }
    u32x4 wo2[32];
#pragma unroll
    for (int it = 0; it < 32; ++it) wo2[it] = ldg16nt(w_o + ((size_t)h * 128 + it * 4 + wave) * 512 + lane * 8);
    if (tid < 128) {
      const int b = tid >> 6, s = tid & 63;
      const float M = xrow_max(row16_max(ml0));
      const float wt = __builtin_amdgcn_exp2f(ml0 - M);
      const float L = xrow_sum(row16_sum(wt * ml1));
      wts[b][s] = wt / L;
    }
    __syncthreads();
    {
      const int b = tid >> 7, c = tid & 127;
      float o = 0.f;
#pragma unroll
      for (int s = 0; s < SB; ++s) o = fmaf(wts[b][s], pv[s], o);
      os[b][c] = o;
    }
    __syncthreads();
    float acc[2][8];
#pragma unroll
    for (int b = 0; b < 2; ++b)
#pragma unroll
      for (int k = 0; k < 8; ++k) acc[b][k] = 0.f;
#pragma unroll
    for (int it = 0; it < 8; ++it) {
      const int c = it * 16 + wave * 4 + rl;
      fma8(acc[0], os[0][c], wv[it]);
      fma8(acc[1], os[1][c], wv[it]);
    }
#pragma unroll
    for (int b = 0; b < 2; ++b)
#pragma unroll
      for (int k = 0; k < 8; ++k) acc[b][k] = xrow_sum(acc[b][k]);
    if (lane < 16) {
#pragma unroll
      for (int b = 0; b < 2; ++b)
#pragma unroll
        for (int k = 0; k < 8; ++k) red[wave][b][dl * 8 + k] = acc[b][k];
    }
    __syncthreads();
    {
      const int b = tid >> 7, d = tid & 127;
      os[b][d] = (red[0][b][d] + red[1][b][d]) + (red[2][b][d] + red[3][b][d]);
    }
    __syncthreads();
    float ac2[2][8];
#pragma unroll
    for (int b = 0; b < 2; ++b)
#pragma unroll
      for (int k = 0; k < 8; ++k) ac2[b][k] = 0.f;
#pragma unroll
    for (int it = 0; it < 32; ++it) {
      const int d = it * 4 + wave;
      fma8(ac2[0], os[0][d], wo2[it]);
      fma8(ac2[1], os[1][d], wo2[it]);
    }
    __syncthreads();
#pragma unroll
    for (int b = 0; b < 2; ++b)
#pragma unroll
      for (int k = 0; k < 8; ++k) red[wave][b][lane * 8 + k] = ac2[b][k];
    __syncthreads();
#pragma unroll
    for (int i = 0; i < 4; ++i) {
      const int idx = tid + 256 * i, b = idx >> 9, m = idx & 511;
      slb[((size_t)j * 2 + b) * 512 + m] = (red[0][b][m] + red[1][b][m]) + (red[2][b][m] + red[3][b][m]);
    }
  }
  STAMP_END(2)
}

__global__ __launch_bounds__(256) void k4_final(const float* __restrict__ sla, const float* __restrict__ slb,
                                                u16* __restrict__ out) {
  const int gid = blockIdx.x * 256 + threadIdx.x;
  const int o = gid >> 3, part = gid & 7;
  const int b = o >> 10, col = o & 1023;
  float v = 0.f;
  if (col < 512) {
    const int cg = col >> 7, c = col & 127;
#pragma unroll
    for (int i = 0; i < 4; ++i) {
      const int h = part * 4 + i;
      v += sla[((size_t)(h * 4 + cg) * 2 + b) * 128 + c];
    }
  } else {
    const int m = col - 512;
#pragma unroll
    for (int i = 0; i < 16; ++i) v += slb[((size_t)(part * 16 + i) * 2 + b) * 512 + m];
  }
  v += dppf<0xB1>(v); v += dppf<0x4E>(v); v += dppf<0x141>(v);
  if (part == 0) out[o] = f2bf(v);
}

#define KMASK 15

extern "C" void kernel_launch(void* const* d_in, const int* in_sizes, int n_in, void* d_out, int out_size,
                              void* d_ws, size_t ws_size, hipStream_t stream) {
  const u16* A_x = (const u16*)d_in[0];
  const u16* A_k = (const u16*)d_in[1];
  const u16* A_v = (const u16*)d_in[2];
  const u16* A_wq = (const u16*)d_in[3];
  const u16* A_wo = (const u16*)d_in[4];
  const u16* B_x = (const u16*)d_in[5];
  const u16* B_ckv = (const u16*)d_in[6];
  const u16* B_wq = (const u16*)d_in[7];
  const u16* B_wuk = (const u16*)d_in[8];
  const u16* B_wuv = (const u16*)d_in[9];
  const u16* B_wo = (const u16*)d_in[10];
  char* ws = (char*)d_ws;
  u16* qA = (u16*)(ws + WS_QA);
  u16* qabs = (u16*)(ws + WS_QABS);
  float* pao = (float*)(ws + WS_PAO);
  float* paml = (float*)(ws + WS_PAML);
  float* pbo = (float*)(ws + WS_PBO);
  float* pbml = (float*)(ws + WS_PBML);
  float* sla = (float*)(ws + WS_SLA);
  float* slb = (float*)(ws + WS_SLB);
  if (KMASK & 1) k1_qproj<<<192, 256, 0, stream>>>(A_x, A_wq, B_x, B_wq, B_wuk, qA, qabs);
  if (KMASK & 2) k2_attn<<<256, 256, 0, stream>>>(A_k, A_v, B_ckv, qA, qabs, pao, paml, pbo, pbml);
  if (KMASK & 4) k3_out<<<256, 256, 0, stream>>>(A_wo, B_wuv, B_wo, pao, paml, pbo, pbml, sla, slb);
  if (KMASK & 8) k4_final<<<64, 256, 0, stream>>>(sla, slb, (u16*)d_out);
}
```
